```python
import torch

def _softmax(x, axis=-1):
    return torch.softmax(x, dim=axis)
def _gelu(x):
    return torch.nn.functional.gelu(x, approximate="tanh")
def _silu(x):
    return torch.nn.functional.silu(x)
def _sigmoid(x):
    return torch.sigmoid(x)
def _layernorm(x, axis=-1, eps=1e-5):
    m = x.mean(dim=axis, keepdim=True)
    v = x.var(dim=axis, keepdim=True, unbiased=False)
    return (x - m) / torch.sqrt(v + eps)
def _scatter_add(tgt, idx, src, axis=0):
    return tgt.clone().index_add_(axis, idx, src)
def _segment_sum(src, seg, n):
    out = torch.zeros((n,) + src.shape[1:], dtype=src.dtype)
    return out.index_add_(0, seg, src)
def _pad(x, pw):
    flat = tuple(v for pair in reversed(pw) for v in pair)
    return torch.nn.functional.pad(x, flat)

kernel_name = 'standalone__rezero__sampling'

def setup_inputs():

    _w = (lambda a: (lambda t: t.to(torch.bfloat16) if t.is_floating_point() else t)(torch.as_tensor(a)))
    B, S, D = 1024, 32, 448
    return {
        "x": _w((lambda *s: torch.randn(*s, dtype=torch.bfloat16) * 0.05)(B, S, D)),
        "fx": _w((lambda *s: torch.randn(*s, dtype=torch.bfloat16) * 0.8)(B, S, D)),
        "alpha": _w((lambda *s: torch.randn(*s, dtype=torch.bfloat16) * 0.8)(1) * 0.1),
    }

def reference(x, fx, alpha):
    return x + alpha * fx
```

```cpp
#include <hip/hip_runtime.h>
#include <hip/hip_bf16.h>
#include <stdint.h>

#define BLOCK 256
#define UNROLL 2

#define LD_AUX 0
#define ST_AUX 0
#define MAXGRID 0

typedef unsigned int u32x4 __attribute__((ext_vector_type(4)));
typedef __bf16 bf16x2_t __attribute__((ext_vector_type(2)));

__device__ __forceinline__ uint32_t pack_bf16(float a, float b) {
    bf16x2_t t = {(__bf16)a, (__bf16)b};
    return __builtin_bit_cast(uint32_t, t);
}

__device__ __forceinline__ uint32_t op2(uint32_t xv, uint32_t fv, float a) {
    float f0 = __uint_as_float(fv << 16), f1 = __uint_as_float(fv & 0xffff0000u);
    float x0 = __uint_as_float(xv << 16), x1 = __uint_as_float(xv & 0xffff0000u);
    uint32_t tu = pack_bf16(a * f0, a * f1);
    float s0 = x0 + __uint_as_float(tu << 16);
    float s1 = x1 + __uint_as_float(tu & 0xffff0000u);
    return pack_bf16(s0, s1);
}

__device__ __forceinline__ u32x4 op8(u32x4 xv, u32x4 fv, float a) {
    u32x4 o;
    o[0] = op2(xv[0], fv[0], a);
    o[1] = op2(xv[1], fv[1], a);
    o[2] = op2(xv[2], fv[2], a);
    o[3] = op2(xv[3], fv[3], a);
    return o;
}

__global__ __launch_bounds__(BLOCK) void rezero_kernel(const void* __restrict__ x, const void* __restrict__ fx,
                                                       const uint16_t* __restrict__ alpha, void* __restrict__ out,
                                                       uint32_t nbytes  ) {
    const float a = __uint_as_float((uint32_t)alpha[0] << 16);
    __amdgpu_buffer_rsrc_t rx = __builtin_amdgcn_make_buffer_rsrc((void*)x, 0, nbytes, 0x00020000);
    __amdgpu_buffer_rsrc_t rf = __builtin_amdgcn_make_buffer_rsrc((void*)fx, 0, nbytes, 0x00020000);
    __amdgpu_buffer_rsrc_t ro = __builtin_amdgcn_make_buffer_rsrc(out, 0, nbytes, 0x00020000);
    const uint32_t chunk = BLOCK * UNROLL * 16;
    {
        const uint32_t base = blockIdx.x * chunk;
        const uint32_t off = base + threadIdx.x * 16;
        u32x4 xv[UNROLL], fv[UNROLL];
#pragma unroll
        for (int u = 0; u < UNROLL; ++u) {
            xv[u] = __builtin_amdgcn_raw_buffer_load_b128(rx, off + u * BLOCK * 16, 0, LD_AUX);
            fv[u] = __builtin_amdgcn_raw_buffer_load_b128(rf, off + u * BLOCK * 16, 0, LD_AUX);
        }
#pragma unroll
        for (int u = 0; u < UNROLL; ++u) {
            u32x4 o = op8(xv[u], fv[u], a);
            __builtin_amdgcn_raw_buffer_store_b128(o, ro, off + u * BLOCK * 16, 0, ST_AUX);
        }
    }
}

__global__ void rezero_tail(const uint16_t* __restrict__ x, const uint16_t* __restrict__ fx,
                            const uint16_t* __restrict__ alpha, uint16_t* __restrict__ out, long start, long n) {
    const float a = __uint_as_float((uint32_t)alpha[0] << 16);
    for (long i = start + blockIdx.x * (long)blockDim.x + threadIdx.x; i < n; i += (long)gridDim.x * blockDim.x) {
        out[i] = (uint16_t)(op2(x[i], fx[i], a) & 0xffffu);
    }
}

extern "C" void kernel_launch(void* const* d_in, const int* in_sizes, int n_in,
                              void* d_out, int out_size, void* d_ws, size_t ws_size,
                              hipStream_t stream) {
    const void* x = d_in[0];
    const void* fx = d_in[1];
    const uint16_t* alpha = (const uint16_t*)d_in[2];
    const long n = out_size;
    const bool aligned = ((((uintptr_t)x) | ((uintptr_t)fx) | ((uintptr_t)d_out)) & 15) == 0;
    long nvec = aligned ? n / 8 : 0;
    if (nvec * 16 > 0xf0000000L) nvec = 0;
    if (nvec > 0) {
        const long per = (long)BLOCK * UNROLL;
        long grid = (nvec + per - 1) / per;
        rezero_kernel<<<(unsigned)grid, BLOCK, 0, stream>>>(x, fx, alpha, d_out, (uint32_t)(nvec * 16));
    }
    if (nvec * 8 < n) {
        long rem = n - nvec * 8;
        long grid = (rem + 255) / 256;
        if (grid > 2048) grid = 2048;
        rezero_tail<<<(unsigned)grid, 256, 0, stream>>>((const uint16_t*)x, (const uint16_t*)fx, alpha,
                                                        (uint16_t*)d_out, nvec * 8, n);
    }
}
```

```cpp
#include <hip/hip_runtime.h>
#include <hip/hip_bf16.h>
#include <stdint.h>

#define STAT 0

#define BLOCK 256
#define UNROLL 2
#define LD_AUX 2
#define ST_AUX 16
#define SCALE 2

typedef unsigned int u32x4 __attribute__((ext_vector_type(4)));
typedef __bf16 bf16x2_t __attribute__((ext_vector_type(2)));

__device__ __forceinline__ uint32_t pack_bf16(float a, float b) {
    bf16x2_t t = {(__bf16)a, (__bf16)b};
    return __builtin_bit_cast(uint32_t, t);
}
__device__ __forceinline__ uint32_t op2(uint32_t xv, uint32_t fv, float a) {
    float f0 = __uint_as_float(fv << 16), f1 = __uint_as_float(fv & 0xffff0000u);
    float x0 = __uint_as_float(xv << 16), x1 = __uint_as_float(xv & 0xffff0000u);
    uint32_t tu = pack_bf16(a * f0, a * f1);
    float s0 = x0 + __uint_as_float(tu << 16);
    float s1 = x1 + __uint_as_float(tu & 0xffff0000u);
    return pack_bf16(s0, s1);
}
__device__ __forceinline__ u32x4 op8(u32x4 xv, u32x4 fv, float a) {
    u32x4 o;
    o[0] = op2(xv[0], fv[0], a); o[1] = op2(xv[1], fv[1], a); o[2] = op2(xv[2], fv[2], a); o[3] = op2(xv[3], fv[3], a);
    return o;
}
__device__ __forceinline__ uint32_t now() {
    unsigned long long t;
    asm volatile("s_memrealtime %0\n\ts_waitcnt lgkmcnt(0)" : "=s"(t) :: "memory");
    return (uint32_t)t;
}

__global__ __launch_bounds__(BLOCK) void rezero_kernel(const void* __restrict__ x, const void* __restrict__ fx,
                                                       const uint32_t* __restrict__ alpha, void* __restrict__ out,
                                                       u32x4* __restrict__ slots, uint32_t nbytes) {
    const uint32_t t0 = now();
    const float a = __uint_as_float(alpha[0] << 16);
    __amdgpu_buffer_rsrc_t rx = __builtin_amdgcn_make_buffer_rsrc((void*)x, 0, nbytes, 0x00020000);
    __amdgpu_buffer_rsrc_t rf = __builtin_amdgcn_make_buffer_rsrc((void*)fx, 0, nbytes, 0x00020000);
    __amdgpu_buffer_rsrc_t ro = __builtin_amdgcn_make_buffer_rsrc(out, 0, nbytes, 0x00020000);
    const uint32_t chunk = BLOCK * UNROLL * 16;
    const uint32_t base = blockIdx.x * chunk;
    const uint32_t off = base + threadIdx.x * 16;
    u32x4 xv[UNROLL], fv[UNROLL];
#pragma unroll
    for (int u = 0; u < UNROLL; ++u) {
        xv[u] = __builtin_amdgcn_raw_buffer_load_b128(rx, off + u * BLOCK * 16, 0, LD_AUX);
        fv[u] = __builtin_amdgcn_raw_buffer_load_b128(rf, off + u * BLOCK * 16, 0, LD_AUX);
    }
    asm volatile("s_waitcnt vmcnt(0)" ::: "memory");
    const uint32_t t1 = now();
#pragma unroll
    for (int u = 0; u < UNROLL; ++u) {
        u32x4 o = op8(xv[u], fv[u], a);
        __builtin_amdgcn_raw_buffer_store_b128(o, ro, off + u * BLOCK * 16, 0, ST_AUX);
    }
    asm volatile("s_waitcnt vmcnt(0)" ::: "memory");
    const uint32_t t2 = now();
    if (threadIdx.x == 0) { u32x4 v = {t0, t1, t2, 0u}; slots[blockIdx.x] = v; }
}

__global__ __launch_bounds__(256) void stat_kernel(const u32x4* __restrict__ slots, int g) {
    const uint32_t tk = now();
    __shared__ int smn[256], smx0[256], smx2[256];
    __shared__ float ssum[256], ssum2[256];
    const uint32_t ref = slots[0][0];
    int mn0 = 1 << 30, mx0 = -(1 << 30), mx2 = -(1 << 30); float sum = 0, sum2 = 0;
    for (int i = threadIdx.x; i < g; i += 256) {
        u32x4 v = slots[i];
        int a0 = (int)(v[0] - ref), a1 = (int)(v[1] - ref), a2 = (int)(v[2] - ref);
        mn0 = min(mn0, a0); mx0 = max(mx0, a0); mx2 = max(mx2, a2); sum += (float)(a1 - a0); sum2 += (float)(a2 - a1);
    }
    smn[threadIdx.x] = mn0; smx0[threadIdx.x] = mx0; smx2[threadIdx.x] = mx2; ssum[threadIdx.x] = sum; ssum2[threadIdx.x] = sum2;
    __syncthreads();
    if (threadIdx.x == 0) {
        for (int i = 1; i < 256; ++i) { mn0 = min(mn0, smn[i]); mx0 = max(mx0, smx0[i]); mx2 = max(mx2, smx2[i]); sum += ssum[i]; sum2 += ssum2[i]; }
        int X = 0;
        if (STAT == 1) X = mx2 - mn0;
        if (STAT == 2) X = mx0 - mn0;
        if (STAT == 3) X = (int)(slots[0][1] - slots[0][0]);
        if (STAT == 4) X = (int)(sum / g);
        if (STAT == 5) X = (int)(tk - ref) - mx2;
        if (STAT == 6) X = (int)(sum2 / g);
        X *= SCALE;
        if (X < 0) X = 0;
        if (X > 30000) X = 30000;
        const uint32_t ts = now();
        while ((int)(now() - ts) < X) { }
    }
}

extern "C" void kernel_launch(void* const* d_in, const int* in_sizes, int n_in,
                              void* d_out, int out_size, void* d_ws, size_t ws_size,
                              hipStream_t stream) {
    const long n = out_size;
    long nvec = n / 8;
    const long per = (long)BLOCK * UNROLL;
    long grid = (nvec + per - 1) / per;
    rezero_kernel<<<(unsigned)grid, BLOCK, 0, stream>>>(d_in[0], d_in[1], (const uint32_t*)d_in[2], d_out, (u32x4*)d_ws, (uint32_t)(nvec * 16));
    stat_kernel<<<1, 256, 0, stream>>>((const u32x4*)d_ws, (int)grid);
}
```

```cpp
#include <hip/hip_runtime.h>
#include <hip/hip_bf16.h>
#include <stdint.h>
#include <stdio.h>

#define BLOCK 256
#define UNROLL 2
#define LD_AUX 2
#define ST_AUX 16

#define MAXGRID 0

typedef unsigned int u32x4 __attribute__((ext_vector_type(4)));
typedef __bf16 bf16x2_t __attribute__((ext_vector_type(2)));

__device__ __forceinline__ uint32_t pack_bf16(float a, float b) {
    bf16x2_t t = {(__bf16)a, (__bf16)b};
    return __builtin_bit_cast(uint32_t, t);
}

__device__ __forceinline__ uint32_t op2(uint32_t xv, uint32_t fv, float a) {
    float f0 = __uint_as_float(fv << 16), f1 = __uint_as_float(fv & 0xffff0000u);
    float x0 = __uint_as_float(xv << 16), x1 = __uint_as_float(xv & 0xffff0000u);
    uint32_t tu = pack_bf16(a * f0, a * f1);
    float s0 = x0 + __uint_as_float(tu << 16);
    float s1 = x1 + __uint_as_float(tu & 0xffff0000u);
    return pack_bf16(s0, s1);
}

__device__ __forceinline__ u32x4 op8(u32x4 xv, u32x4 fv, float a) {
    u32x4 o;
    o[0] = op2(xv[0], fv[0], a);
    o[1] = op2(xv[1], fv[1], a);
    o[2] = op2(xv[2], fv[2], a);
    o[3] = op2(xv[3], fv[3], a);
    return o;
}

__global__ __launch_bounds__(BLOCK) void rezero_kernel(const void* __restrict__ x, const void* __restrict__ fx,
                                                       const uint16_t* __restrict__ alpha, void* __restrict__ out,
                                                       uint32_t nbytes  ) {
    const float a = __uint_as_float((uint32_t)alpha[0] << 16);
    __amdgpu_buffer_rsrc_t rx = __builtin_amdgcn_make_buffer_rsrc((void*)x, 0, nbytes, 0x00020000);
    __amdgpu_buffer_rsrc_t rf = __builtin_amdgcn_make_buffer_rsrc((void*)fx, 0, nbytes, 0x00020000);
    __amdgpu_buffer_rsrc_t ro = __builtin_amdgcn_make_buffer_rsrc(out, 0, nbytes, 0x00020000);
    const uint32_t chunk = BLOCK * UNROLL * 16;
    {
        const uint32_t base = blockIdx.x * chunk;
        const uint32_t off = base + threadIdx.x * 16;
        u32x4 xv[UNROLL], fv[UNROLL];
#pragma unroll
        for (int u = 0; u < UNROLL; ++u) {
            xv[u] = __builtin_amdgcn_raw_buffer_load_b128(rx, off + u * BLOCK * 16, 0, LD_AUX);
            fv[u] = __builtin_amdgcn_raw_buffer_load_b128(rf, off + u * BLOCK * 16, 0, LD_AUX);
        }
#pragma unroll
        for (int u = 0; u < UNROLL; ++u) {
            u32x4 o = op8(xv[u], fv[u], a);
            __builtin_amdgcn_raw_buffer_store_b128(o, ro, off + u * BLOCK * 16, 0, ST_AUX);
        }
    }
}

__global__ void rezero_tail(const uint16_t* __restrict__ x, const uint16_t* __restrict__ fx,
                            const uint16_t* __restrict__ alpha, uint16_t* __restrict__ out, long start, long n) {
    const float a = __uint_as_float((uint32_t)alpha[0] << 16);
    for (long i = start + blockIdx.x * (long)blockDim.x + threadIdx.x; i < n; i += (long)gridDim.x * blockDim.x) {
        out[i] = (uint16_t)(op2(x[i], fx[i], a) & 0xffffu);
    }
}

extern "C" void kernel_launch(void* const* d_in, const int* in_sizes, int n_in,
                              void* d_out, int out_size, void* d_ws, size_t ws_size,
                              hipStream_t stream) {
    const void* x = d_in[0];
    const void* fx = d_in[1];
    const uint16_t* alpha = (const uint16_t*)d_in[2];
    const long n = out_size;
    fprintf(stderr, "PTRS x=%p fx=%p alpha=%p out=%p ws=%p ws_size=%zu\n", d_in[0], d_in[1], d_in[2], d_out, d_ws, ws_size);
    const bool aligned = ((((uintptr_t)x) | ((uintptr_t)fx) | ((uintptr_t)d_out)) & 15) == 0;
    long nvec = aligned ? n / 8 : 0;
    if (nvec * 16 > 0xf0000000L) nvec = 0;
    if (nvec > 0) {
        const long per = (long)BLOCK * UNROLL;
        long grid = (nvec + per - 1) / per;
        rezero_kernel<<<(unsigned)grid, BLOCK, 0, stream>>>(x, fx, alpha, d_out, (uint32_t)(nvec * 16));
    }
    if (nvec * 8 < n) {
        long rem = n - nvec * 8;
        long grid = (rem + 255) / 256;
        if (grid > 2048) grid = 2048;
        rezero_tail<<<(unsigned)grid, 256, 0, stream>>>((const uint16_t*)x, (const uint16_t*)fx, alpha,
                                                        (uint16_t*)d_out, nvec * 8, n);
    }
}
```

```cpp
#include <hip/hip_runtime.h>
#include <hip/hip_bf16.h>
#include <stdint.h>

#define UNROLL 4
#define BLOCK 256

typedef unsigned int u32x4 __attribute__((ext_vector_type(4)));
typedef float f32x2 __attribute__((ext_vector_type(2)));
typedef __bf16 bf16x2_t __attribute__((ext_vector_type(2)));

__device__ __forceinline__ uint32_t pack_bf16(float lo, float hi) {
    bf16x2_t t = {(__bf16)lo, (__bf16)hi};
    return __builtin_bit_cast(uint32_t, t);
}
__device__ __forceinline__ float round_bf16(float p) {
    uint32_t r;
    asm("v_cvt_pk_bf16_f32 %0, 0, %1" : "=v"(r) : "v"(p));
    return __uint_as_float(r);
}
__device__ __forceinline__ uint32_t op2(uint32_t xv, uint32_t fv, float a) {
    f32x2 f = {__uint_as_float(fv << 16), __uint_as_float(fv & 0xffff0000u)};
    f32x2 xx = {__uint_as_float(xv << 16), __uint_as_float(xv & 0xffff0000u)};
    f32x2 p = f * a;
    f32x2 t = {round_bf16(p[0]), round_bf16(p[1])};
    f32x2 s = xx + t;
    return pack_bf16(s[0], s[1]);
}
__device__ __forceinline__ u32x4 op8(u32x4 xv, u32x4 fv, float a) {
    u32x4 o;
    o[0] = op2(xv[0], fv[0], a); o[1] = op2(xv[1], fv[1], a); o[2] = op2(xv[2], fv[2], a); o[3] = op2(xv[3], fv[3], a);
    return o;
}

__global__ __launch_bounds__(BLOCK) void rezero_kernel(const char* __restrict__ x, const char* __restrict__ fx,
                                                       const uint32_t* __restrict__ alpha, char* __restrict__ out) {
    const uint32_t off = blockIdx.x * (BLOCK * UNROLL * 16) + (threadIdx.x >> 6) * (UNROLL * 1024) + (threadIdx.x & 63) * 16;
    u32x4 xv[UNROLL], fv[UNROLL];
#pragma unroll
    for (int u = 0; u < UNROLL; ++u) {
        xv[u] = __builtin_nontemporal_load((const u32x4*)(x + off + u * 1024));
        fv[u] = __builtin_nontemporal_load((const u32x4*)(fx + off + u * 1024));
    }
    const float a = __uint_as_float(alpha[0] << 16);
    __amdgpu_buffer_rsrc_t ro = __builtin_amdgcn_make_buffer_rsrc(out, 0, 0xffffffffu, 0x00020000);
#pragma unroll
    for (int u = 0; u < UNROLL; ++u) {
        u32x4 o = op8(xv[u], fv[u], a);
        __builtin_amdgcn_raw_buffer_store_b128(o, ro, off + u * 1024, 0, 16);
    }
}

__global__ void rezero_tail(const uint16_t* __restrict__ x, const uint16_t* __restrict__ fx,
                            const uint16_t* __restrict__ alpha, uint16_t* __restrict__ out, long start, long n) {
    const float a = __uint_as_float((uint32_t)alpha[0] << 16);
    for (long i = start + blockIdx.x * (long)blockDim.x + threadIdx.x; i < n; i += (long)gridDim.x * blockDim.x)
        out[i] = (uint16_t)(op2(x[i], fx[i], a) & 0xffffu);
}

extern "C" void kernel_launch(void* const* d_in, const int* in_sizes, int n_in,
                              void* d_out, int out_size, void* d_ws, size_t ws_size,
                              hipStream_t stream) {
    const void* x = d_in[0];
    const void* fx = d_in[1];
    const void* alpha = d_in[2];
    const long n = out_size;
    const bool aligned = (((((uintptr_t)x) | ((uintptr_t)fx) | ((uintptr_t)d_out)) & 15) == 0) && ((((uintptr_t)alpha) & 3) == 0);
    const long per = (long)BLOCK * UNROLL * 8;
    long grid = aligned ? n / per : 0;
    if (grid * per * 2 > 0xf0000000L) grid = 0xf0000000L / (per * 2);
    if (grid > 0)
        rezero_kernel<<<(unsigned)grid, BLOCK, 0, stream>>>((const char*)x, (const char*)fx, (const uint32_t*)alpha, (char*)d_out);
    const long done = grid * per;
    if (done < n) {
        long g = (n - done + 255) / 256;
        if (g > 4096) g = 4096;
        rezero_tail<<<(unsigned)g, 256, 0, stream>>>((const uint16_t*)x, (const uint16_t*)fx, (const uint16_t*)alpha,
                                                     (uint16_t*)d_out, done, n);
    }
}
```

```cpp
#include <hip/hip_runtime.h>
#include <hip/hip_bf16.h>
#include <stdint.h>

namespace {

constexpr int kBlock = 256;
constexpr int kUnroll = 4;

typedef unsigned int u32x4 __attribute__((ext_vector_type(4)));
typedef float f32x2 __attribute__((ext_vector_type(2)));
typedef __bf16 bf16x2_t __attribute__((ext_vector_type(2)));

__device__ __forceinline__ uint32_t pack_bf16(float lo, float hi) {
    bf16x2_t t = {(__bf16)lo, (__bf16)hi};
    return __builtin_bit_cast(uint32_t, t);
}

__device__ __forceinline__ float round_bf16(float p) {
    uint32_t r;
    asm("v_cvt_pk_bf16_f32 %0, 0, %1" : "=v"(r) : "v"(p));
    return __uint_as_float(r);
}

__device__ __forceinline__ uint32_t rezero2(uint32_t xv, uint32_t fv, float a) {
    const f32x2 f = {__uint_as_float(fv << 16), __uint_as_float(fv & 0xffff0000u)};
    const f32x2 r = {__uint_as_float(xv << 16), __uint_as_float(xv & 0xffff0000u)};
    const f32x2 p = f * a;
    const f32x2 t = {round_bf16(p[0]), round_bf16(p[1])};
    const f32x2 s = r + t;
    return pack_bf16(s[0], s[1]);
}

__device__ __forceinline__ u32x4 rezero8(u32x4 xv, u32x4 fv, float a) {
    u32x4 o;
    o[0] = rezero2(xv[0], fv[0], a);
    o[1] = rezero2(xv[1], fv[1], a);
    o[2] = rezero2(xv[2], fv[2], a);
    o[3] = rezero2(xv[3], fv[3], a);
    return o;
}

}

__global__ __launch_bounds__(kBlock) void rezero_main_kernel(const char* __restrict__ x,
                                                             const char* __restrict__ fx,
                                                             const uint32_t* __restrict__ alpha32,
                                                             char* __restrict__ out,
                                                             uint32_t nbytes) {
    const uint32_t off = blockIdx.x * (kBlock * kUnroll * 16) + (threadIdx.x >> 6) * (kUnroll * 1024) +
                         (threadIdx.x & 63) * 16;
    u32x4 xv[kUnroll], fv[kUnroll];
#pragma unroll
    for (int u = 0; u < kUnroll; ++u) {
        xv[u] = __builtin_nontemporal_load((const u32x4*)(x + off + u * 1024));
        fv[u] = __builtin_nontemporal_load((const u32x4*)(fx + off + u * 1024));
    }
    const float a = __uint_as_float(alpha32[0] << 16);
    const __amdgpu_buffer_rsrc_t ro = __builtin_amdgcn_make_buffer_rsrc(out, 0, nbytes, 0x00020000);
#pragma unroll
    for (int u = 0; u < kUnroll; ++u) {
        const u32x4 o = rezero8(xv[u], fv[u], a);
        __builtin_amdgcn_raw_buffer_store_b128(o, ro, off + u * 1024, 0,   16);
    }
}

__global__ void rezero_tail_kernel(const uint16_t* __restrict__ x, const uint16_t* __restrict__ fx,
                                   const uint16_t* __restrict__ alpha, uint16_t* __restrict__ out,
                                   long start, long n) {
    const float a = __uint_as_float((uint32_t)alpha[0] << 16);
    const long stride = (long)gridDim.x * blockDim.x;
    for (long i = start + blockIdx.x * (long)blockDim.x + threadIdx.x; i < n; i += stride)
        out[i] = (uint16_t)(rezero2(x[i], fx[i], a) & 0xffffu);
}

extern "C" void kernel_launch(void* const* d_in, const int* in_sizes, int n_in,
                              void* d_out, int out_size, void* d_ws, size_t ws_size,
                              hipStream_t stream) {
    (void)in_sizes; (void)n_in; (void)d_ws; (void)ws_size;
    const void* x = d_in[0];
    const void* fx = d_in[1];
    const void* alpha = d_in[2];
    const long n = out_size;

    const bool aligned = (((((uintptr_t)x) | ((uintptr_t)fx) | ((uintptr_t)d_out)) & 15) == 0) &&
                         ((((uintptr_t)alpha) & 3) == 0);
    const long per_block = (long)kBlock * kUnroll * 8;
    long grid = aligned ? n / per_block : 0;
    const long max_grid = 0xf0000000L / (per_block * 2);
    if (grid > max_grid) grid = max_grid;
    const long done = grid * per_block;

    if (grid > 0)
        rezero_main_kernel<<<(unsigned)grid, kBlock, 0, stream>>>(
            (const char*)x, (const char*)fx, (const uint32_t*)alpha, (char*)d_out, (uint32_t)(done * 2));
    if (done < n) {
        long g = (n - done + 255) / 256;
        if (g > 4096) g = 4096;
        rezero_tail_kernel<<<(unsigned)g, 256, 0, stream>>>((const uint16_t*)x, (const uint16_t*)fx,
                                                            (const uint16_t*)alpha, (uint16_t*)d_out, done, n);
    }
}
```

```cpp
#include <hip/hip_runtime.h>
#include <hip/hip_bf16.h>
#include <stdint.h>

namespace {

constexpr int kBlock = 256;
constexpr int kUnroll = 4;

typedef unsigned int u32x4 __attribute__((ext_vector_type(4)));
typedef float f32x2 __attribute__((ext_vector_type(2)));
typedef __bf16 bf16x2_t __attribute__((ext_vector_type(2)));

__device__ __forceinline__ uint32_t pack_bf16(float lo, float hi) {
    bf16x2_t t = {(__bf16)lo, (__bf16)hi};
    return __builtin_bit_cast(uint32_t, t);
}

__device__ __forceinline__ float round_bf16(float p) {
    uint32_t r;
    asm("v_cvt_pk_bf16_f32 %0, 0, %1" : "=v"(r) : "v"(p));
    return __uint_as_float(r);
}

__device__ __forceinline__ uint32_t rezero2(uint32_t xv, uint32_t fv, float a) {
    const f32x2 f = {__uint_as_float(fv << 16), __uint_as_float(fv & 0xffff0000u)};
    const f32x2 r = {__uint_as_float(xv << 16), __uint_as_float(xv & 0xffff0000u)};
    const f32x2 p = f * a;
    const f32x2 t = {round_bf16(p[0]), round_bf16(p[1])};
    const f32x2 s = r + t;
    return pack_bf16(s[0], s[1]);
}

__device__ __forceinline__ u32x4 rezero8(u32x4 xv, u32x4 fv, float a) {
    u32x4 o;
    o[0] = rezero2(xv[0], fv[0], a);
    o[1] = rezero2(xv[1], fv[1], a);
    o[2] = rezero2(xv[2], fv[2], a);
    o[3] = rezero2(xv[3], fv[3], a);
    return o;
}

}

__global__ __launch_bounds__(kBlock) void rezero_main_kernel(const char* __restrict__ x,
                                                             const char* __restrict__ fx,
                                                             const uint32_t* __restrict__ alpha32,
                                                             char* __restrict__ out,
                                                             uint32_t nbytes) {
    const uint32_t off = blockIdx.x * (kBlock * kUnroll * 16) + (threadIdx.x >> 6) * (kUnroll * 1024) +
                         (threadIdx.x & 63) * 16;
    u32x4 xv[kUnroll], fv[kUnroll];
#pragma unroll
    for (int u = 0; u < kUnroll; ++u) {
        xv[u] = __builtin_nontemporal_load((const u32x4*)(x + off + u * 1024));
        fv[u] = __builtin_nontemporal_load((const u32x4*)(fx + off + u * 1024));
    }
    const float a = __uint_as_float(alpha32[0] << 16);
    const __amdgpu_buffer_rsrc_t ro = __builtin_amdgcn_make_buffer_rsrc(out, 0, nbytes, 0x00020000);
#pragma unroll
    for (int u = 0; u < kUnroll; ++u) {
        const u32x4 o = rezero8(xv[u], fv[u], a);
        __builtin_amdgcn_raw_buffer_store_b128(o, ro, off + u * 1024, 0,   16);
    }
}

#define KSLEEP 80
constexpr int kSleep = KSLEEP;
constexpr uint32_t kThr = 60;
constexpr unsigned long long kMagic = 0x5a17c3e9b2d46f00ull;

__global__ __launch_bounds__(256) void rezero_numa_kernel(const char* __restrict__ x, const char* __restrict__ fx,
                                                          const uint32_t* __restrict__ alpha32, char* __restrict__ out,
                                                          unsigned long long* __restrict__ ws, uint32_t nbytes, uint32_t npairs) {
    const uint32_t b = blockIdx.x, side = (b >> 2) & 1, pair = (b >> 3) * 4 + (b & 3);
    const uint32_t w = __builtin_amdgcn_readfirstlane(threadIdx.x >> 6), lane = threadIdx.x & 63;
    const uint32_t page2 = npairs >> 7;
    const uint32_t arr = b & 3, pg = (b >> 3) & 7, first = (b >> 6) & 1;
    const uint32_t po = (page2 + pg) * 2097152u + (b & 127) * 16384u + ((b >> 7) & 1) * 4096u + first * 8192u;
    const bool prober = b < 256 && w == 0 && arr < 3 && po + 16384u <= nbytes;
    uint32_t v0, v1;
    if (prober) {
        const char* base = arr == 0 ? x : (arr == 1 ? fx : out);
        asm volatile("global_load_dword %0, %2, %4 nt\n\tglobal_load_dword %1, %3, %4 nt"
                     : "=&v"(v0), "=&v"(v1) : "v"(po), "v"(po ^ 8192u), "s"(base) : "memory");
    }
    __builtin_amdgcn_sched_barrier(0);
    const uint32_t inoff = w * 2048 + lane * 16;
    const uint32_t dflt = side ^ (pair & 1);
    const uint32_t off1 = pair * 16384u + dflt * 8192u + inoff;
    u32x4 xv[2], fv[2], x2[2], f2[2];
#pragma unroll
    for (int u = 0; u < 2; ++u) {
        xv[u] = __builtin_nontemporal_load((const u32x4*)(x + off1 + u * 1024));
        fv[u] = __builtin_nontemporal_load((const u32x4*)(fx + off1 + u * 1024));
    }
    __builtin_amdgcn_sched_barrier(0);
    if (prober) {
        unsigned long long t1, t2, tr;
        asm volatile("s_waitcnt vmcnt(5)\n\ts_memtime %0\n\ts_waitcnt vmcnt(4)\n\ts_memtime %1\n\ts_memrealtime %2\n\ts_waitcnt lgkmcnt(0)"
                     : "=&s"(t1), "=&s"(t2), "=&s"(tr), "+v"(v0), "+v"(v1) :: "memory");
        const uint32_t d = (uint32_t)(t2 - t1);
        const uint32_t pos = d >= kThr ? 1u : 0u;
        const unsigned long long v = ((unsigned long long)(uint32_t)tr << 32) | (pos << 1) | (first ^ side);
        if (lane == 0) __hip_atomic_store(ws + pg * 32 + (b & 7) + ((b >> 6) << 3), v, __ATOMIC_RELAXED, __HIP_MEMORY_SCOPE_AGENT);
    }
    __builtin_amdgcn_sched_barrier(0);
    const uint32_t t0 = (uint32_t)__builtin_amdgcn_s_memrealtime();
    const float a = __uint_as_float(alpha32[0] << 16);
    const __amdgpu_buffer_rsrc_t ro = __builtin_amdgcn_make_buffer_rsrc(out, 0, nbytes, 0x00020000);
    const uint32_t u2 = npairs + pair;
    const uint32_t pg2 = (u2 >> 7) - page2;
    const unsigned long long* vp = ws + pg2 * 32 + (lane & 31);
    __builtin_amdgcn_s_sleep(kSleep);
    int score = 0; bool decided = false;
    {
        const unsigned long long vote = __hip_atomic_load(vp, __ATOMIC_RELAXED, __HIP_MEMORY_SCOPE_AGENT);
        const int age = (int)((uint32_t)(vote >> 32) - t0);
        const bool good = lane < 32 && (lane & 3) < 3 && age > -800 && age < 20000 && (vote & 2);
        const uint32_t m1 = (uint32_t)__ballot(good && (vote & 1)), m0 = (uint32_t)__ballot(good && !(vote & 1));
        uint32_t known = 0; score = 0;
#pragma unroll
        for (int ar = 0; ar < 3; ++ar) {
            const uint32_t am = 0x11111111u << ar;
            const int d = __builtin_popcount(m0 & am) - __builtin_popcount(m1 & am);
            score += d > 0 ? 1 : (d < 0 ? -1 : 0); known += d != 0;
        }
        decided = known == 3 || score >= 2 || score <= -2;
    }
    uint32_t par = decided ? ((score > 0 ? 0u : 1u) ^ side) : dflt;
    unsigned long long* pw = ws + 4096 + (size_t)(pair * 4 + w) * 8;
    unsigned long long old;
    {
        const unsigned long long mine = kMagic | (side << 1) | par;
        asm volatile("s_mov_b64 exec, 1\n\tglobal_atomic_swap_x2 %0, %1, %2, %3 sc0\n\ts_mov_b64 exec, -1"
                     : "=&v"(old) : "v"(0u), "v"(mine), "s"(pw) : "memory");
    }
    __builtin_amdgcn_sched_barrier(0);
    uint32_t off2 = u2 * 16384u + par * 8192u + inoff;
#pragma unroll
    for (int u = 0; u < 2; ++u) {
        x2[u] = __builtin_nontemporal_load((const u32x4*)(x + off2 + u * 1024));
        f2[u] = __builtin_nontemporal_load((const u32x4*)(fx + off2 + u * 1024));
    }
    __builtin_amdgcn_sched_barrier(0);
#pragma unroll
    for (int u = 0; u < 2; ++u) {
        u32x4 o = rezero8(xv[u], fv[u], a);
        __builtin_amdgcn_raw_buffer_store_b128(o, ro, off1 + u * 1024, 0, 16);
    }
    __builtin_amdgcn_sched_barrier(0);
    asm volatile("s_waitcnt vmcnt(6)" : "+v"(old) :: "memory");
    __builtin_amdgcn_sched_barrier(0);
    const uint32_t olo = __builtin_amdgcn_readfirstlane((uint32_t)old), ohi = __builtin_amdgcn_readfirstlane((uint32_t)(old >> 32));
    const bool second = ohi == (uint32_t)(kMagic >> 32) && (olo & ~3u) == (uint32_t)kMagic && ((olo >> 1) & 1) != side;
    const bool conflict = second && (olo & 1) == par;
    if (conflict) {
        off2 ^= 8192u;
#pragma unroll
        for (int u = 0; u < 2; ++u) {
            x2[u] = __builtin_nontemporal_load((const u32x4*)(x + off2 + u * 1024));
            f2[u] = __builtin_nontemporal_load((const u32x4*)(fx + off2 + u * 1024));
        }
    }
    if (second && lane == 0) __hip_atomic_store(pw, 0ull, __ATOMIC_RELAXED, __HIP_MEMORY_SCOPE_AGENT);
#pragma unroll
    for (int u = 0; u < 2; ++u) {
        u32x4 o = rezero8(x2[u], f2[u], a);
        __builtin_amdgcn_raw_buffer_store_b128(o, ro, off2 + u * 1024, 0, 16);
    }
}

__global__ void rezero_tail_kernel(const uint16_t* __restrict__ x, const uint16_t* __restrict__ fx,
                                   const uint16_t* __restrict__ alpha, uint16_t* __restrict__ out,
                                   long start, long n) {
    const float a = __uint_as_float((uint32_t)alpha[0] << 16);
    const long stride = (long)gridDim.x * blockDim.x;
    for (long i = start + blockIdx.x * (long)blockDim.x + threadIdx.x; i < n; i += stride)
        out[i] = (uint16_t)(rezero2(x[i], fx[i], a) & 0xffffu);
}

extern "C" void kernel_launch(void* const* d_in, const int* in_sizes, int n_in,
                              void* d_out, int out_size, void* d_ws, size_t ws_size,
                              hipStream_t stream) {
    (void)in_sizes; (void)n_in; (void)d_ws; (void)ws_size;
    const void* x = d_in[0];
    const void* fx = d_in[1];
    const void* alpha = d_in[2];
    const long n = out_size;

    const bool aligned = (((((uintptr_t)x) | ((uintptr_t)fx) | ((uintptr_t)d_out)) & 15) == 0) &&
                         ((((uintptr_t)alpha) & 3) == 0);
    {
        const long bytes = n * 2;
        const long npairs = bytes / 32768;
        const bool ok = aligned && bytes % 131072 == 0 && npairs >= 128 && bytes < 0x70000000L &&
                        (((2 * npairs - 1) >> 7) - (npairs >> 7)) <= 7 &&
                        (((uintptr_t)d_ws) & 63) == 0 && ws_size >= (size_t)(4096 + npairs * 32) * 8;
        if (ok) {
            rezero_numa_kernel<<<(unsigned)(2 * npairs), 256, 0, stream>>>(
                (const char*)x, (const char*)fx, (const uint32_t*)alpha, (char*)d_out, (unsigned long long*)d_ws,
                (uint32_t)bytes, (uint32_t)npairs);
            return;
        }
    }
    const long per_block = (long)kBlock * kUnroll * 8;
    long grid = aligned ? n / per_block : 0;
    const long max_grid = 0xf0000000L / (per_block * 2);
    if (grid > max_grid) grid = max_grid;
    const long done = grid * per_block;

    if (grid > 0)
        rezero_main_kernel<<<(unsigned)grid, kBlock, 0, stream>>>(
            (const char*)x, (const char*)fx, (const uint32_t*)alpha, (char*)d_out, (uint32_t)(done * 2));
    if (done < n) {
        long g = (n - done + 255) / 256;
        if (g > 4096) g = 4096;
        rezero_tail_kernel<<<(unsigned)g, 256, 0, stream>>>((const uint16_t*)x, (const uint16_t*)fx,
                                                            (const uint16_t*)alpha, (uint16_t*)d_out, done, n);
    }
}
```

```cpp
#include <hip/hip_runtime.h>
#include <hip/hip_bf16.h>
#include <stdint.h>

namespace {

constexpr int kBlock = 256;
constexpr int kUnroll = 4;

typedef unsigned int u32x4 __attribute__((ext_vector_type(4)));
typedef float f32x2 __attribute__((ext_vector_type(2)));
typedef __bf16 bf16x2_t __attribute__((ext_vector_type(2)));

__device__ __forceinline__ uint32_t pack_bf16(float lo, float hi) {
    bf16x2_t t = {(__bf16)lo, (__bf16)hi};
    return __builtin_bit_cast(uint32_t, t);
}

__device__ __forceinline__ float round_bf16(float p) {
    uint32_t r;
    asm("v_cvt_pk_bf16_f32 %0, 0, %1" : "=v"(r) : "v"(p));
    return __uint_as_float(r);
}

__device__ __forceinline__ uint32_t rezero2(uint32_t xv, uint32_t fv, float a) {
    const f32x2 f = {__uint_as_float(fv << 16), __uint_as_float(fv & 0xffff0000u)};
    const f32x2 r = {__uint_as_float(xv << 16), __uint_as_float(xv & 0xffff0000u)};
    const f32x2 p = f * a;
    const f32x2 t = {round_bf16(p[0]), round_bf16(p[1])};
    const f32x2 s = r + t;
    return pack_bf16(s[0], s[1]);
}

__device__ __forceinline__ u32x4 rezero8(u32x4 xv, u32x4 fv, float a) {
    u32x4 o;
    o[0] = rezero2(xv[0], fv[0], a);
    o[1] = rezero2(xv[1], fv[1], a);
    o[2] = rezero2(xv[2], fv[2], a);
    o[3] = rezero2(xv[3], fv[3], a);
    return o;
}

}

__global__ __launch_bounds__(kBlock) void rezero_main_kernel(const char* __restrict__ x,
                                                             const char* __restrict__ fx,
                                                             const uint32_t* __restrict__ alpha32,
                                                             char* __restrict__ out,
                                                             uint32_t nbytes) {
    const uint32_t off = blockIdx.x * (kBlock * kUnroll * 16) + (threadIdx.x >> 6) * (kUnroll * 1024) +
                         (threadIdx.x & 63) * 16;
    u32x4 xv[kUnroll], fv[kUnroll];
#pragma unroll
    for (int u = 0; u < kUnroll; ++u) {
        xv[u] = __builtin_nontemporal_load((const u32x4*)(x + off + u * 1024));
        fv[u] = __builtin_nontemporal_load((const u32x4*)(fx + off + u * 1024));
    }
    const float a = __uint_as_float(alpha32[0] << 16);
    const __amdgpu_buffer_rsrc_t ro = __builtin_amdgcn_make_buffer_rsrc(out, 0, nbytes, 0x00020000);
#pragma unroll
    for (int u = 0; u < kUnroll; ++u) {
        const u32x4 o = rezero8(xv[u], fv[u], a);
        __builtin_amdgcn_raw_buffer_store_b128(o, ro, off + u * 1024, 0,   16);
    }
}

#define KSLEEP 70
namespace {
constexpr int kSleep = KSLEEP;
constexpr uint32_t kThr = 60;
constexpr unsigned long long kMagic = 0x5a17c3e9b2d46f00ull;
constexpr uint32_t kPairBase = 4096;
}

__global__ __launch_bounds__(256) void rezero_numa_kernel(const char* __restrict__ x, const char* __restrict__ fx,
                                                          const uint32_t* __restrict__ alpha32, char* __restrict__ out,
                                                          unsigned long long* __restrict__ ws, uint32_t nbytes,
                                                          uint32_t npairs) {
    __shared__ uint32_t s_dec[2];
    const uint32_t b = blockIdx.x, side = (b >> 2) & 1, pair = (b >> 3) * 4 + (b & 3);
    const uint32_t w = __builtin_amdgcn_readfirstlane(threadIdx.x >> 6), lane = threadIdx.x & 63;
    const uint32_t page2 = npairs >> 7;

    const uint32_t grp = (b >> 6) & 3, pg = (b >> 3) & 7, first = grp & 1;
    const uint32_t arr = (b & 3) == 3 ? (grp == 3 ? 0u : grp) : (b & 3);
    const uint32_t po = (page2 + pg) * 2097152u + (b & 127) * 16384u + (grp >> 1) * 4096u + first * 8192u;
    const bool prober = b < 256 && w == 0 && po + 16384u <= nbytes;
    uint32_t v0, v1;
    if (prober) {
        const char* base = arr == 0 ? x : (arr == 1 ? fx : out);
        asm volatile("global_load_dword %0, %2, %4 nt\n\tglobal_load_dword %1, %3, %4 nt"
                     : "=&v"(v0), "=&v"(v1) : "v"(po), "v"(po ^ 8192u), "s"(base) : "memory");
    }
    __builtin_amdgcn_sched_barrier(0);

    const uint32_t inoff = w * 2048 + lane * 16;
    const uint32_t dflt = side ^ (pair & 1);
    const uint32_t off1 = pair * 16384u + dflt * 8192u + inoff;
    u32x4 xv[2], fv[2], x2[2], f2[2];
#pragma unroll
    for (int u = 0; u < 2; ++u) {
        xv[u] = __builtin_nontemporal_load((const u32x4*)(x + off1 + u * 1024));
        fv[u] = __builtin_nontemporal_load((const u32x4*)(fx + off1 + u * 1024));
    }
    __builtin_amdgcn_sched_barrier(0);
    if (prober) {
        unsigned long long t1, t2, tr;
        asm volatile("s_waitcnt vmcnt(5)\n\ts_memtime %0\n\ts_waitcnt vmcnt(4)\n\ts_memtime %1\n\ts_memrealtime %2\n\t"
                     "s_waitcnt lgkmcnt(0)"
                     : "=&s"(t1), "=&s"(t2), "=&s"(tr), "+v"(v0), "+v"(v1) :: "memory");
        const uint32_t pos = (uint32_t)(t2 - t1) >= kThr ? 2u : 0u;
        const unsigned long long v = ((unsigned long long)(uint32_t)tr << 32) | pos | (first ^ side);
        if (lane == 0)
            __hip_atomic_store(ws + pg * 32 + (b & 7) + (grp << 3), v, __ATOMIC_RELAXED, __HIP_MEMORY_SCOPE_AGENT);
    }
    __builtin_amdgcn_sched_barrier(0);

    const float a = __uint_as_float(alpha32[0] << 16);
    const __amdgpu_buffer_rsrc_t ro = __builtin_amdgcn_make_buffer_rsrc(out, 0, nbytes, 0x00020000);
    const uint32_t u2 = npairs + pair;

    if (w == 0) {
        const uint32_t t0 = (uint32_t)__builtin_amdgcn_s_memrealtime();
        const unsigned long long* vp = ws + ((u2 >> 7) - page2) * 32 + (lane & 31);
        __builtin_amdgcn_s_sleep(kSleep);
        const unsigned long long vote = __hip_atomic_load(vp, __ATOMIC_RELAXED, __HIP_MEMORY_SCOPE_AGENT);
        const uint32_t agep = (uint32_t)(vote >> 32) - t0 + 800u;
        const uint32_t code = agep < 20800u ? ((uint32_t)vote & 3u) : 0u;
        const uint32_t m1 = (uint32_t)__ballot(code == 3u), m0 = (uint32_t)__ballot(code == 2u);
        const uint32_t am[3] = {0x99111199u, 0x2222aa22u, 0x44cc4444u};
        int score = 0, known = 0;
#pragma unroll
        for (int ar = 0; ar < 3; ++ar) {
            const int d = __builtin_popcount(m0 & am[ar]) - __builtin_popcount(m1 & am[ar]);
            score += d > 0 ? 1 : (d < 0 ? -1 : 0);
            known += d != 0;
        }
        const bool decided = known == 3 || score >= 2 || score <= -2;
        const uint32_t p0 = decided ? ((score > 0 ? 0u : 1u) ^ side) : dflt;
        if (lane == 0) s_dec[0] = p0;
        asm volatile("s_waitcnt lgkmcnt(0)" ::: "memory");
    }
    __builtin_amdgcn_s_barrier();
    asm volatile("" ::: "memory");
    const uint32_t par = __builtin_amdgcn_readfirstlane(s_dec[0]);

    unsigned long long* pw = ws + kPairBase + (size_t)pair * 8;
    unsigned long long old = 0;
    if (w == 0) {
        const unsigned long long mine = kMagic | (side << 1) | par;
        asm volatile("s_mov_b64 exec, 1\n\tglobal_atomic_swap_x2 %0, %1, %2, %3 sc0\n\ts_mov_b64 exec, -1"
                     : "=&v"(old) : "v"(0u), "v"(mine), "s"(pw) : "memory");
    }
    __builtin_amdgcn_sched_barrier(0);
    uint32_t off2 = u2 * 16384u + par * 8192u + inoff;
#pragma unroll
    for (int u = 0; u < 2; ++u) {
        x2[u] = __builtin_nontemporal_load((const u32x4*)(x + off2 + u * 1024));
        f2[u] = __builtin_nontemporal_load((const u32x4*)(fx + off2 + u * 1024));
    }
    __builtin_amdgcn_sched_barrier(0);
#pragma unroll
    for (int u = 0; u < 2; ++u) {
        const u32x4 o = rezero8(xv[u], fv[u], a);
        __builtin_amdgcn_raw_buffer_store_b128(o, ro, off1 + u * 1024, 0,   16);
    }
    __builtin_amdgcn_sched_barrier(0);
    asm volatile("s_waitcnt vmcnt(6)" : "+v"(old) :: "memory");
    __builtin_amdgcn_sched_barrier(0);
    const uint32_t olo = __builtin_amdgcn_readfirstlane((uint32_t)old);
    const uint32_t ohi = __builtin_amdgcn_readfirstlane((uint32_t)(old >> 32));
    const bool second = ohi == (uint32_t)(kMagic >> 32) && (olo & ~3u) == (uint32_t)kMagic && ((olo >> 1) & 1) != side;
    if (w == 0) {
        if (lane == 0) s_dec[1] = (second && (olo & 1) == par) ? 1u : 0u;
        asm volatile("s_waitcnt lgkmcnt(0)" ::: "memory");
    }
    __builtin_amdgcn_s_barrier();
    asm volatile("" ::: "memory");
    if (__builtin_amdgcn_readfirstlane(s_dec[1]) != 0) {
        off2 ^= 8192u;
#pragma unroll
        for (int u = 0; u < 2; ++u) {
            x2[u] = __builtin_nontemporal_load((const u32x4*)(x + off2 + u * 1024));
            f2[u] = __builtin_nontemporal_load((const u32x4*)(fx + off2 + u * 1024));
        }
    }
    if (second && lane == 0) __hip_atomic_store(pw, 0ull, __ATOMIC_RELAXED, __HIP_MEMORY_SCOPE_AGENT);
#pragma unroll
    for (int u = 0; u < 2; ++u) {
        const u32x4 o = rezero8(x2[u], f2[u], a);
        __builtin_amdgcn_raw_buffer_store_b128(o, ro, off2 + u * 1024, 0,   16);
    }
}

__global__ void rezero_tail_kernel(const uint16_t* __restrict__ x, const uint16_t* __restrict__ fx,
                                   const uint16_t* __restrict__ alpha, uint16_t* __restrict__ out,
                                   long start, long n) {
    const float a = __uint_as_float((uint32_t)alpha[0] << 16);
    const long stride = (long)gridDim.x * blockDim.x;
    for (long i = start + blockIdx.x * (long)blockDim.x + threadIdx.x; i < n; i += stride)
        out[i] = (uint16_t)(rezero2(x[i], fx[i], a) & 0xffffu);
}

extern "C" void kernel_launch(void* const* d_in, const int* in_sizes, int n_in,
                              void* d_out, int out_size, void* d_ws, size_t ws_size,
                              hipStream_t stream) {
    (void)in_sizes; (void)n_in;
    const void* x = d_in[0];
    const void* fx = d_in[1];
    const void* alpha = d_in[2];
    const long n = out_size;

    const bool aligned = (((((uintptr_t)x) | ((uintptr_t)fx) | ((uintptr_t)d_out)) & 15) == 0) &&
                         ((((uintptr_t)alpha) & 3) == 0);
    {
        const long bytes = n * 2;
        const long npairs = bytes / 32768;
        const bool ok = aligned && bytes % 131072 == 0 && npairs >= 128 && bytes < 0x70000000L &&
                        (((2 * npairs - 1) >> 7) - (npairs >> 7)) <= 7 &&
                        (((uintptr_t)d_ws) & 63) == 0 && ws_size >= (size_t)(kPairBase + npairs * 8) * 8;
        if (ok) {
            rezero_numa_kernel<<<(unsigned)(2 * npairs), 256, 0, stream>>>(
                (const char*)x, (const char*)fx, (const uint32_t*)alpha, (char*)d_out, (unsigned long long*)d_ws,
                (uint32_t)bytes, (uint32_t)npairs);
            return;
        }
    }
    const long per_block = (long)kBlock * kUnroll * 8;
    long grid = aligned ? n / per_block : 0;
    const long max_grid = 0xf0000000L / (per_block * 2);
    if (grid > max_grid) grid = max_grid;
    const long done = grid * per_block;

    if (grid > 0)
        rezero_main_kernel<<<(unsigned)grid, kBlock, 0, stream>>>(
            (const char*)x, (const char*)fx, (const uint32_t*)alpha, (char*)d_out, (uint32_t)(done * 2));
    if (done < n) {
        long g = (n - done + 255) / 256;
        if (g > 4096) g = 4096;
        rezero_tail_kernel<<<(unsigned)g, 256, 0, stream>>>((const uint16_t*)x, (const uint16_t*)fx,
                                                            (const uint16_t*)alpha, (uint16_t*)d_out, done, n);
    }
}
```

```cpp
#include <hip/hip_runtime.h>
#include <hip/hip_bf16.h>
#include <stdint.h>

namespace {

constexpr int kBlock = 256;
constexpr int kUnroll = 4;

typedef unsigned int u32x4 __attribute__((ext_vector_type(4)));
typedef float f32x2 __attribute__((ext_vector_type(2)));
typedef __bf16 bf16x2_t __attribute__((ext_vector_type(2)));

__device__ __forceinline__ uint32_t pack_bf16(float lo, float hi) {
    bf16x2_t t = {(__bf16)lo, (__bf16)hi};
    return __builtin_bit_cast(uint32_t, t);
}

__device__ __forceinline__ float round_bf16(float p) {
    uint32_t r;
    asm("v_cvt_pk_bf16_f32 %0, 0, %1" : "=v"(r) : "v"(p));
    return __uint_as_float(r);
}

__device__ __forceinline__ uint32_t rezero2(uint32_t xv, uint32_t fv, float a) {
    const f32x2 f = {__uint_as_float(fv << 16), __uint_as_float(fv & 0xffff0000u)};
    const f32x2 r = {__uint_as_float(xv << 16), __uint_as_float(xv & 0xffff0000u)};
    const f32x2 p = f * a;
    const f32x2 t = {round_bf16(p[0]), round_bf16(p[1])};
    const f32x2 s = r + t;
    return pack_bf16(s[0], s[1]);
}

__device__ __forceinline__ u32x4 rezero8(u32x4 xv, u32x4 fv, float a) {
    u32x4 o;
    o[0] = rezero2(xv[0], fv[0], a);
    o[1] = rezero2(xv[1], fv[1], a);
    o[2] = rezero2(xv[2], fv[2], a);
    o[3] = rezero2(xv[3], fv[3], a);
    return o;
}

}

__global__ __launch_bounds__(kBlock) void rezero_main_kernel(const char* __restrict__ x,
                                                             const char* __restrict__ fx,
                                                             const uint32_t* __restrict__ alpha32,
                                                             char* __restrict__ out,
                                                             uint32_t nbytes) {
    const uint32_t off = blockIdx.x * (kBlock * kUnroll * 16) + (threadIdx.x >> 6) * (kUnroll * 1024) +
                         (threadIdx.x & 63) * 16;
    u32x4 xv[kUnroll], fv[kUnroll];
#pragma unroll
    for (int u = 0; u < kUnroll; ++u) {
        xv[u] = __builtin_nontemporal_load((const u32x4*)(x + off + u * 1024));
        fv[u] = __builtin_nontemporal_load((const u32x4*)(fx + off + u * 1024));
    }
    const float a = __uint_as_float(alpha32[0] << 16);
    const __amdgpu_buffer_rsrc_t ro = __builtin_amdgcn_make_buffer_rsrc(out, 0, nbytes, 0x00020000);
#pragma unroll
    for (int u = 0; u < kUnroll; ++u) {
        const u32x4 o = rezero8(xv[u], fv[u], a);
        __builtin_amdgcn_raw_buffer_store_b128(o, ro, off + u * 1024, 0,   16);
    }
}


namespace {
constexpr int kSleep = 70;
constexpr uint32_t kThr = 60;
constexpr unsigned long long kMagic = 0x5a17c3e9b2d46f00ull;
constexpr uint32_t kPairBase = 4096;
}

__global__ __launch_bounds__(256) void rezero_numa_kernel(const char* __restrict__ x, const char* __restrict__ fx,
                                                          const uint32_t* __restrict__ alpha32, char* __restrict__ out,
                                                          unsigned long long* __restrict__ ws, uint32_t nbytes,
                                                          uint32_t npairs) {
    __shared__ uint32_t s_dec[2];
    const uint32_t b = blockIdx.x, side = (b >> 2) & 1, pair = (b >> 3) * 4 + (b & 3);
    const uint32_t w = __builtin_amdgcn_readfirstlane(threadIdx.x >> 6), lane = threadIdx.x & 63;
    const uint32_t page2 = npairs >> 7;

    const uint32_t grp = (b >> 6) & 3, pg = (b >> 3) & 7, first = grp & 1;
    const uint32_t arr = (b & 3) == 3 ? (grp == 3 ? 0u : grp) : (b & 3);
    const uint32_t po = (page2 + pg) * 2097152u + (b & 127) * 16384u + (grp >> 1) * 4096u + first * 8192u;
    const bool prober = b < 256 && w == 0 && po + 16384u <= nbytes;
    uint32_t v0, v1;
    if (prober) {
        const char* base = arr == 0 ? x : (arr == 1 ? fx : out);
        asm volatile("global_load_dword %0, %2, %4 nt\n\tglobal_load_dword %1, %3, %4 nt"
                     : "=&v"(v0), "=&v"(v1) : "v"(po), "v"(po ^ 8192u), "s"(base) : "memory");
    }
    __builtin_amdgcn_sched_barrier(0);

    const uint32_t inoff = w * 2048 + lane * 16;
    const uint32_t dflt = side ^ (pair & 1);
    const uint32_t off1 = pair * 16384u + dflt * 8192u + inoff;
    u32x4 xv[2], fv[2], x2[2], f2[2];
#pragma unroll
    for (int u = 0; u < 2; ++u) {
        xv[u] = __builtin_nontemporal_load((const u32x4*)(x + off1 + u * 1024));
        fv[u] = __builtin_nontemporal_load((const u32x4*)(fx + off1 + u * 1024));
    }
    __builtin_amdgcn_sched_barrier(0);
    if (prober) {
        unsigned long long t1, t2, tr;
        asm volatile("s_waitcnt vmcnt(5)\n\ts_memtime %0\n\ts_waitcnt vmcnt(4)\n\ts_memtime %1\n\ts_memrealtime %2\n\t"
                     "s_waitcnt lgkmcnt(0)"
                     : "=&s"(t1), "=&s"(t2), "=&s"(tr), "+v"(v0), "+v"(v1) :: "memory");
        const uint32_t pos = (uint32_t)(t2 - t1) >= kThr ? 2u : 0u;
        const unsigned long long v = ((unsigned long long)(uint32_t)tr << 32) | pos | (first ^ side);
        if (lane == 0)
            __hip_atomic_store(ws + pg * 32 + (b & 7) + (grp << 3), v, __ATOMIC_RELAXED, __HIP_MEMORY_SCOPE_AGENT);
    }
    __builtin_amdgcn_sched_barrier(0);

    const float a = __uint_as_float(alpha32[0] << 16);
    const __amdgpu_buffer_rsrc_t ro = __builtin_amdgcn_make_buffer_rsrc(out, 0, nbytes, 0x00020000);
    const uint32_t u2 = npairs + pair;

    if (w == 0) {
        const uint32_t t0 = (uint32_t)__builtin_amdgcn_s_memrealtime();
        const unsigned long long* vp = ws + ((u2 >> 7) - page2) * 32 + (lane & 31);
        __builtin_amdgcn_s_sleep(kSleep);
        const unsigned long long vote = __hip_atomic_load(vp, __ATOMIC_RELAXED, __HIP_MEMORY_SCOPE_AGENT);
        const uint32_t agep = (uint32_t)(vote >> 32) - t0 + 400u;
        const uint32_t code = agep < 20400u ? ((uint32_t)vote & 3u) : 0u;
        const uint32_t m1 = (uint32_t)__ballot(code == 3u), m0 = (uint32_t)__ballot(code == 2u);
        const uint32_t am[3] = {0x99111199u, 0x2222aa22u, 0x44cc4444u};
        int score = 0, known = 0;
#pragma unroll
        for (int ar = 0; ar < 3; ++ar) {
            const int d = __builtin_popcount(m0 & am[ar]) - __builtin_popcount(m1 & am[ar]);
            score += d > 0 ? 1 : (d < 0 ? -1 : 0);
            known += d != 0;
        }
        const bool decided = known == 3 || score >= 2 || score <= -2;
        const uint32_t p0 = decided ? ((score > 0 ? 0u : 1u) ^ side) : dflt;
        if (lane == 0) s_dec[0] = p0;
        asm volatile("s_waitcnt lgkmcnt(0)" ::: "memory");
    }
    __builtin_amdgcn_s_barrier();
    asm volatile("" ::: "memory");
    const uint32_t par = __builtin_amdgcn_readfirstlane(s_dec[0]);

    unsigned long long* pw = ws + kPairBase + (size_t)pair * 8;
    unsigned long long old = 0;
    if (w == 0) {
        const unsigned long long mine = kMagic | (side << 1) | par;
        asm volatile("s_mov_b64 exec, 1\n\tglobal_atomic_swap_x2 %0, %1, %2, %3 sc0\n\ts_mov_b64 exec, -1"
                     : "=&v"(old) : "v"(0u), "v"(mine), "s"(pw) : "memory");
    }
    __builtin_amdgcn_sched_barrier(0);
    uint32_t off2 = u2 * 16384u + par * 8192u + inoff;
#pragma unroll
    for (int u = 0; u < 2; ++u) {
        x2[u] = __builtin_nontemporal_load((const u32x4*)(x + off2 + u * 1024));
        f2[u] = __builtin_nontemporal_load((const u32x4*)(fx + off2 + u * 1024));
    }
    __builtin_amdgcn_sched_barrier(0);
#pragma unroll
    for (int u = 0; u < 2; ++u) {
        const u32x4 o = rezero8(xv[u], fv[u], a);
        __builtin_amdgcn_raw_buffer_store_b128(o, ro, off1 + u * 1024, 0,   16);
    }
    __builtin_amdgcn_sched_barrier(0);
    asm volatile("s_waitcnt vmcnt(6)" : "+v"(old) :: "memory");
    __builtin_amdgcn_sched_barrier(0);
    const uint32_t olo = __builtin_amdgcn_readfirstlane((uint32_t)old);
    const uint32_t ohi = __builtin_amdgcn_readfirstlane((uint32_t)(old >> 32));
    const bool second = ohi == (uint32_t)(kMagic >> 32) && (olo & ~3u) == (uint32_t)kMagic && ((olo >> 1) & 1) != side;
    if (w == 0) {
        if (lane == 0) s_dec[1] = (second && (olo & 1) == par) ? 1u : 0u;
        asm volatile("s_waitcnt lgkmcnt(0)" ::: "memory");
    }
    __builtin_amdgcn_s_barrier();
    asm volatile("" ::: "memory");
    if (__builtin_amdgcn_readfirstlane(s_dec[1]) != 0) {
        off2 ^= 8192u;
#pragma unroll
        for (int u = 0; u < 2; ++u) {
            x2[u] = __builtin_nontemporal_load((const u32x4*)(x + off2 + u * 1024));
            f2[u] = __builtin_nontemporal_load((const u32x4*)(fx + off2 + u * 1024));
        }
    }
    if (second && lane == 0) __hip_atomic_store(pw, 0ull, __ATOMIC_RELAXED, __HIP_MEMORY_SCOPE_AGENT);
#pragma unroll
    for (int u = 0; u < 2; ++u) {
        const u32x4 o = rezero8(x2[u], f2[u], a);
        __builtin_amdgcn_raw_buffer_store_b128(o, ro, off2 + u * 1024, 0,   16);
    }
}

__global__ void rezero_tail_kernel(const uint16_t* __restrict__ x, const uint16_t* __restrict__ fx,
                                   const uint16_t* __restrict__ alpha, uint16_t* __restrict__ out,
                                   long start, long n) {
    const float a = __uint_as_float((uint32_t)alpha[0] << 16);
    const long stride = (long)gridDim.x * blockDim.x;
    for (long i = start + blockIdx.x * (long)blockDim.x + threadIdx.x; i < n; i += stride)
        out[i] = (uint16_t)(rezero2(x[i], fx[i], a) & 0xffffu);
}

extern "C" void kernel_launch(void* const* d_in, const int* in_sizes, int n_in,
                              void* d_out, int out_size, void* d_ws, size_t ws_size,
                              hipStream_t stream) {
    (void)in_sizes; (void)n_in;
    const void* x = d_in[0];
    const void* fx = d_in[1];
    const void* alpha = d_in[2];
    const long n = out_size;

    const bool aligned = (((((uintptr_t)x) | ((uintptr_t)fx) | ((uintptr_t)d_out)) & 15) == 0) &&
                         ((((uintptr_t)alpha) & 3) == 0);
    {
        const long bytes = n * 2;
        const long npairs = bytes / 32768;
        const bool ok = aligned && bytes % 131072 == 0 && npairs >= 512 && bytes < 0x70000000L &&
                        (((2 * npairs - 1) >> 7) - (npairs >> 7)) <= 7 &&
                        (((uintptr_t)d_ws) & 63) == 0 && ws_size >= (size_t)(kPairBase + npairs * 8) * 8;
        if (ok) {
            rezero_numa_kernel<<<(unsigned)(2 * npairs), 256, 0, stream>>>(
                (const char*)x, (const char*)fx, (const uint32_t*)alpha, (char*)d_out, (unsigned long long*)d_ws,
                (uint32_t)bytes, (uint32_t)npairs);
            return;
        }
    }
    const long per_block = (long)kBlock * kUnroll * 8;
    long grid = aligned ? n / per_block : 0;
    const long max_grid = 0xf0000000L / (per_block * 2);
    if (grid > max_grid) grid = max_grid;
    const long done = grid * per_block;

    if (grid > 0)
        rezero_main_kernel<<<(unsigned)grid, kBlock, 0, stream>>>(
            (const char*)x, (const char*)fx, (const uint32_t*)alpha, (char*)d_out, (uint32_t)(done * 2));
    if (done < n) {
        long g = (n - done + 255) / 256;
        if (g > 4096) g = 4096;
        rezero_tail_kernel<<<(unsigned)g, 256, 0, stream>>>((const uint16_t*)x, (const uint16_t*)fx,
                                                            (const uint16_t*)alpha, (uint16_t*)d_out, done, n);
    }
}
```

```cpp
#include <hip/hip_runtime.h>
#include <hip/hip_bf16.h>
#include <stdint.h>

namespace {

constexpr int kBlock = 256;
constexpr int kUnroll = 4;

typedef unsigned int u32x4 __attribute__((ext_vector_type(4)));
typedef float f32x2 __attribute__((ext_vector_type(2)));
typedef __bf16 bf16x2_t __attribute__((ext_vector_type(2)));

__device__ __forceinline__ uint32_t pack_bf16(float lo, float hi) {
    bf16x2_t t = {(__bf16)lo, (__bf16)hi};
    return __builtin_bit_cast(uint32_t, t);
}

__device__ __forceinline__ float round_bf16(float p) {
    uint32_t r;
    asm("v_cvt_pk_bf16_f32 %0, 0, %1" : "=v"(r) : "v"(p));
    return __uint_as_float(r);
}

__device__ __forceinline__ uint32_t rezero2(uint32_t xv, uint32_t fv, float a) {
    const f32x2 f = {__uint_as_float(fv << 16), __uint_as_float(fv & 0xffff0000u)};
    const f32x2 r = {__uint_as_float(xv << 16), __uint_as_float(xv & 0xffff0000u)};
    const f32x2 p = f * a;
    const f32x2 t = {round_bf16(p[0]), round_bf16(p[1])};
    const f32x2 s = r + t;
    return pack_bf16(s[0], s[1]);
}

__device__ __forceinline__ u32x4 rezero8(u32x4 xv, u32x4 fv, float a) {
    u32x4 o;
    o[0] = rezero2(xv[0], fv[0], a);
    o[1] = rezero2(xv[1], fv[1], a);
    o[2] = rezero2(xv[2], fv[2], a);
    o[3] = rezero2(xv[3], fv[3], a);
    return o;
}

}

__global__ __launch_bounds__(kBlock) void rezero_main_kernel(const char* __restrict__ x,
                                                             const char* __restrict__ fx,
                                                             const uint32_t* __restrict__ alpha32,
                                                             char* __restrict__ out,
                                                             uint32_t nbytes) {
    const uint32_t off = blockIdx.x * (kBlock * kUnroll * 16) + (threadIdx.x >> 6) * (kUnroll * 1024) +
                         (threadIdx.x & 63) * 16;
    u32x4 xv[kUnroll], fv[kUnroll];
#pragma unroll
    for (int u = 0; u < kUnroll; ++u) {
        xv[u] = __builtin_nontemporal_load((const u32x4*)(x + off + u * 1024));
        fv[u] = __builtin_nontemporal_load((const u32x4*)(fx + off + u * 1024));
    }
    const float a = __uint_as_float(alpha32[0] << 16);
    const __amdgpu_buffer_rsrc_t ro = __builtin_amdgcn_make_buffer_rsrc(out, 0, nbytes, 0x00020000);
#pragma unroll
    for (int u = 0; u < kUnroll; ++u) {
        const u32x4 o = rezero8(xv[u], fv[u], a);
        __builtin_amdgcn_raw_buffer_store_b128(o, ro, off + u * 1024, 0,   16);
    }
}


namespace {
constexpr int kSleep = 70;
constexpr uint32_t kThr = 60;
constexpr unsigned long long kMagic = 0x5a17c3e9b2d46f00ull;
constexpr uint32_t kPairBase = 4096;
}

__global__ __launch_bounds__(256) void rezero_numa_kernel(const char* __restrict__ x, const char* __restrict__ fx,
                                                          const uint32_t* __restrict__ alpha32, char* __restrict__ out,
                                                          unsigned long long* __restrict__ ws, uint32_t nbytes,
                                                          uint32_t npairs) {
    __shared__ uint32_t s_dec[2];
    const uint32_t b = blockIdx.x, side = (b >> 2) & 1, pair = (b >> 3) * 4 + (b & 3);
    const uint32_t w = __builtin_amdgcn_readfirstlane(threadIdx.x >> 6), lane = threadIdx.x & 63;
    const uint32_t page2 = npairs >> 7;

    const uint32_t grp = (b >> 6) & 3, pg = (b >> 3) & 7, first = grp & 1;
    const uint32_t arr = (b & 3) == 3 ? (grp == 3 ? 0u : grp) : (b & 3);
    const uint32_t po = (page2 + pg) * 2097152u + (b & 127) * 16384u + (grp >> 1) * 4096u + first * 8192u;
    const bool prober = b < 256 && w == 0 && po + 16384u <= nbytes;
    uint32_t v0, v1;
    if (prober) {
        const char* base = arr == 0 ? x : (arr == 1 ? fx : out);
        asm volatile("global_load_dword %0, %2, %4 nt\n\tglobal_load_dword %1, %3, %4 nt"
                     : "=&v"(v0), "=&v"(v1) : "v"(po), "v"(po ^ 8192u), "s"(base) : "memory");
    }
    __builtin_amdgcn_sched_barrier(0);

    const uint32_t inoff = w * 2048 + lane * 16;
    const uint32_t dflt = side ^ (pair & 1);
    const uint32_t off1 = pair * 16384u + dflt * 8192u + inoff;
    u32x4 xv[2], fv[2], x2[2], f2[2];
#pragma unroll
    for (int u = 0; u < 2; ++u) {
        xv[u] = __builtin_nontemporal_load((const u32x4*)(x + off1 + u * 1024));
        fv[u] = __builtin_nontemporal_load((const u32x4*)(fx + off1 + u * 1024));
    }
    __builtin_amdgcn_sched_barrier(0);
    if (prober) {
        unsigned long long t1, t2, tr;
        asm volatile("s_waitcnt vmcnt(5)\n\ts_memtime %0\n\ts_waitcnt vmcnt(4)\n\ts_memtime %1\n\ts_memrealtime %2\n\t"
                     "s_waitcnt lgkmcnt(0)"
                     : "=&s"(t1), "=&s"(t2), "=&s"(tr), "+v"(v0), "+v"(v1) :: "memory");
        const uint32_t pos = (uint32_t)(t2 - t1) >= kThr ? 2u : 0u;
        const unsigned long long v = ((unsigned long long)(uint32_t)tr << 32) | pos | (first ^ side);
        if (lane == 0)
            __hip_atomic_store(ws + pg * 32 + (b & 7) + (grp << 3), v, __ATOMIC_RELAXED, __HIP_MEMORY_SCOPE_AGENT);
    }
    __builtin_amdgcn_sched_barrier(0);

    const float a = __uint_as_float(alpha32[0] << 16);
    const __amdgpu_buffer_rsrc_t ro = __builtin_amdgcn_make_buffer_rsrc(out, 0, nbytes, 0x00020000);
    const uint32_t u2 = npairs + pair;

    if (w == 0) {
        const uint32_t t0 = (uint32_t)__builtin_amdgcn_s_memrealtime();
        const unsigned long long* vp = ws + ((u2 >> 7) - page2) * 32 + (lane & 31);
        __builtin_amdgcn_s_sleep(kSleep);
        const unsigned long long vote = __hip_atomic_load(vp, __ATOMIC_RELAXED, __HIP_MEMORY_SCOPE_AGENT);
        const uint32_t agep = (uint32_t)(vote >> 32) - t0 + 800u;
        const uint32_t code = agep < 20800u ? ((uint32_t)vote & 3u) : 0u;
        const uint32_t m1 = (uint32_t)__ballot(code == 3u), m0 = (uint32_t)__ballot(code == 2u);
        const uint32_t am[3] = {0x99111199u, 0x2222aa22u, 0x44cc4444u};
        int score = 0, known = 0;
#pragma unroll
        for (int ar = 0; ar < 3; ++ar) {
            const int d = __builtin_popcount(m0 & am[ar]) - __builtin_popcount(m1 & am[ar]);
            score += d > 0 ? 1 : (d < 0 ? -1 : 0);
            known += d != 0;
        }
        const bool decided = known == 3 || score >= 2 || score <= -2;
        const uint32_t p0 = decided ? ((score > 0 ? 0u : 1u) ^ side) : dflt;
        if (lane == 0) s_dec[0] = p0;
        asm volatile("s_waitcnt lgkmcnt(0)" ::: "memory");
    }
    __builtin_amdgcn_s_barrier();
    asm volatile("" ::: "memory");
    const uint32_t par = __builtin_amdgcn_readfirstlane(s_dec[0]);

    unsigned long long* pw = ws + kPairBase + (size_t)pair * 8;
    unsigned long long old = 0;
    if (w == 0) {
        const unsigned long long mine = kMagic | (side << 1) | par;
        asm volatile("s_mov_b64 exec, 1\n\tglobal_atomic_swap_x2 %0, %1, %2, %3 sc0\n\ts_mov_b64 exec, -1"
                     : "=&v"(old) : "v"(0u), "v"(mine), "s"(pw) : "memory");
    }
    __builtin_amdgcn_sched_barrier(0);
    uint32_t off2 = u2 * 16384u + par * 8192u + inoff;
#pragma unroll
    for (int u = 0; u < 2; ++u) {
        x2[u] = __builtin_nontemporal_load((const u32x4*)(x + off2 + u * 1024));
        f2[u] = __builtin_nontemporal_load((const u32x4*)(fx + off2 + u * 1024));
    }
    __builtin_amdgcn_sched_barrier(0);
#pragma unroll
    for (int u = 0; u < 2; ++u) {
        const u32x4 o = rezero8(xv[u], fv[u], a);
        __builtin_amdgcn_raw_buffer_store_b128(o, ro, off1 + u * 1024, 0,   16);
    }
    __builtin_amdgcn_sched_barrier(0);
    asm volatile("s_waitcnt vmcnt(4)" : "+v"(old) :: "memory");
    __builtin_amdgcn_sched_barrier(0);
    const uint32_t olo = __builtin_amdgcn_readfirstlane((uint32_t)old);
    const uint32_t ohi = __builtin_amdgcn_readfirstlane((uint32_t)(old >> 32));
    const bool second = ohi == (uint32_t)(kMagic >> 32) && (olo & ~3u) == (uint32_t)kMagic && ((olo >> 1) & 1) != side;
    if (w == 0) {
        if (lane == 0) s_dec[1] = (second && (olo & 1) == par) ? 1u : 0u;
        asm volatile("s_waitcnt lgkmcnt(0)" ::: "memory");
    }
    __builtin_amdgcn_s_barrier();
    asm volatile("" ::: "memory");
    if (__builtin_amdgcn_readfirstlane(s_dec[1]) != 0) {
        off2 ^= 8192u;
#pragma unroll
        for (int u = 0; u < 2; ++u) {
            x2[u] = __builtin_nontemporal_load((const u32x4*)(x + off2 + u * 1024));
            f2[u] = __builtin_nontemporal_load((const u32x4*)(fx + off2 + u * 1024));
        }
    }
    if (second && lane == 0) __hip_atomic_store(pw, 0ull, __ATOMIC_RELAXED, __HIP_MEMORY_SCOPE_AGENT);
#pragma unroll
    for (int u = 0; u < 2; ++u) {
        const u32x4 o = rezero8(x2[u], f2[u], a);
        __builtin_amdgcn_raw_buffer_store_b128(o, ro, off2 + u * 1024, 0,   16);
    }
}

__global__ void rezero_tail_kernel(const uint16_t* __restrict__ x, const uint16_t* __restrict__ fx,
                                   const uint16_t* __restrict__ alpha, uint16_t* __restrict__ out,
                                   long start, long n) {
    const float a = __uint_as_float((uint32_t)alpha[0] << 16);
    const long stride = (long)gridDim.x * blockDim.x;
    for (long i = start + blockIdx.x * (long)blockDim.x + threadIdx.x; i < n; i += stride)
        out[i] = (uint16_t)(rezero2(x[i], fx[i], a) & 0xffffu);
}

extern "C" void kernel_launch(void* const* d_in, const int* in_sizes, int n_in,
                              void* d_out, int out_size, void* d_ws, size_t ws_size,
                              hipStream_t stream) {
    (void)in_sizes; (void)n_in;
    const void* x = d_in[0];
    const void* fx = d_in[1];
    const void* alpha = d_in[2];
    const long n = out_size;

    const bool aligned = (((((uintptr_t)x) | ((uintptr_t)fx) | ((uintptr_t)d_out)) & 15) == 0) &&
                         ((((uintptr_t)alpha) & 3) == 0);
    {
        const long bytes = n * 2;
        const long npairs = bytes / 32768;
        const bool ok = aligned && bytes % 131072 == 0 && npairs >= 768 && bytes < 0x70000000L &&
                        (((2 * npairs - 1) >> 7) - (npairs >> 7)) <= 7 &&
                        (((uintptr_t)d_ws) & 63) == 0 && ws_size >= (size_t)(kPairBase + npairs * 8) * 8;
        if (ok) {
            rezero_numa_kernel<<<(unsigned)(2 * npairs), 256, 0, stream>>>(
                (const char*)x, (const char*)fx, (const uint32_t*)alpha, (char*)d_out, (unsigned long long*)d_ws,
                (uint32_t)bytes, (uint32_t)npairs);
            return;
        }
    }
    const long per_block = (long)kBlock * kUnroll * 8;
    long grid = aligned ? n / per_block : 0;
    const long max_grid = 0xf0000000L / (per_block * 2);
    if (grid > max_grid) grid = max_grid;
    const long done = grid * per_block;

    if (grid > 0)
        rezero_main_kernel<<<(unsigned)grid, kBlock, 0, stream>>>(
            (const char*)x, (const char*)fx, (const uint32_t*)alpha, (char*)d_out, (uint32_t)(done * 2));
    if (done < n) {
        long g = (n - done + 255) / 256;
        if (g > 4096) g = 4096;
        rezero_tail_kernel<<<(unsigned)g, 256, 0, stream>>>((const uint16_t*)x, (const uint16_t*)fx,
                                                            (const uint16_t*)alpha, (uint16_t*)d_out, done, n);
    }
}
```
